```python
import torch

def _softmax(x, axis=-1):
    return torch.softmax(x, dim=axis)
def _gelu(x):
    return torch.nn.functional.gelu(x, approximate="tanh")
def _silu(x):
    return torch.nn.functional.silu(x)
def _sigmoid(x):
    return torch.sigmoid(x)
def _layernorm(x, axis=-1, eps=1e-5):
    m = x.mean(dim=axis, keepdim=True)
    v = x.var(dim=axis, keepdim=True, unbiased=False)
    return (x - m) / torch.sqrt(v + eps)
def _scatter_add(tgt, idx, src, axis=0):
    return tgt.clone().index_add_(axis, idx, src)
def _segment_sum(src, seg, n):
    out = torch.zeros((n,) + src.shape[1:], dtype=src.dtype)
    return out.index_add_(0, seg, src)
def _pad(x, pw):
    flat = tuple(v for pair in reversed(pw) for v in pair)
    return torch.nn.functional.pad(x, flat)

kernel_name = 'recurrent_scan__ssm_conv_h3__geglu_ffn__prompt'

def setup_inputs():

    _w = (lambda a: (lambda t: t.to(torch.bfloat16) if t.is_floating_point() else t)(torch.as_tensor(a)))
    def _A_setup():
        B, S, D = 1, 2048, 512
        ks = 4
        return {
            "q": _w((lambda *s: torch.randn(*s, dtype=torch.bfloat16) * 0.05)(B, S, D)),
            "k": _w((lambda *s: torch.randn(*s, dtype=torch.bfloat16) * 0.2)(B, S, D)),
            "v": _w((lambda *s: torch.randn(*s, dtype=torch.bfloat16) * 0.2)(B, S, D)),
            "shift_k": _w((lambda *s: torch.randn(*s, dtype=torch.bfloat16) * 0.2)(D, ks) * 0.5),
            "ssm_k": _w((lambda *s: torch.randn(*s, dtype=torch.bfloat16) * 0.2)(D, S) * (1.0 / S ** 0.5)),
            "ssm_D": _w((lambda *s: torch.randn(*s, dtype=torch.bfloat16) * 0.2)(D)),
        }
    _A_in = _A_setup()

    def _B_setup():
        B, S, D, F = 1, 2048, 512, 1792
        return {
            "x": _w((lambda *s: torch.randn(*s, dtype=torch.bfloat16) * 0.05)(B, S, D)),
            "w_gate": _w((lambda *s: torch.randn(*s, dtype=torch.bfloat16) * 0.1)(D, F)),
            "w_up": _w((lambda *s: torch.randn(*s, dtype=torch.bfloat16) * 0.1)(D, F)),
            "w_down": _w((lambda *s: torch.randn(*s, dtype=torch.bfloat16) * 0.1)(F, D)),
        }
    _B_in = _B_setup()

    return {
        "A_q": _A_in["q"],
        "A_k": _A_in["k"],
        "A_v": _A_in["v"],
        "A_shift_k": _A_in["shift_k"],
        "A_ssm_k": _A_in["ssm_k"],
        "A_ssm_D": _A_in["ssm_D"],
        "B_x": _B_in["x"],
        "B_w_gate": _B_in["w_gate"],
        "B_w_up": _B_in["w_up"],
        "B_w_down": _B_in["w_down"],
    }

def reference(A_q, A_k, A_v, A_shift_k, A_ssm_k, A_ssm_D, B_x, B_w_gate, B_w_up, B_w_down):
    def _A_ref(q, k, v, shift_k, ssm_k, ssm_D):
        B, S, D = q.shape
        ks = shift_k.shape[-1]
        k_pad = _pad(k, ((0, 0), (ks - 1, 0), (0, 0)))
        k_shift = torch.zeros_like(k)
        for t in range(ks):
            k_shift = k_shift + k_pad[:, t:t + S, :] * shift_k[:, ks - 1 - t][None, None, :]
        kv = k_shift * v
        L = 2 * S
        kv_pad = _pad(kv, ((0, 0), (0, S), (0, 0)))
        kf_pad = _pad(ssm_k, ((0, 0), (0, S)))
        kv_f = (lambda x, axes=(-2,-1): torch.fft.rfft2(x.float(), dim=axes))(kv_pad[:, None, :, :], axes=(1, 2))
        kf_f = (lambda x, axes=(-2,-1): torch.fft.rfft2(x.float(), dim=axes))(kf_pad[:, None, :], axes=(1, 2))
        kf_f = (lambda x, *d: x.permute(*d))(kf_f, 1, 2, 0)
        y_f = kv_f * kf_f[None, :, :, :]
        y = (lambda x, s, axes=(-2,-1): torch.fft.irfft2(x, s=s, dim=axes).to(torch.bfloat16))(y_f, (1, L), axes=(1, 2))[:, 0, :S, :]
        y = y + kv * ssm_D[None, None, :]
        return q * y

    def _B_ref(x, w_gate, w_up, w_down):
        return (_gelu(x @ w_gate) * (x @ w_up)) @ w_down

    x = A_q
    xn = _layernorm(x, axis=-1)
    s = _A_ref(xn, A_k, A_v, A_shift_k, A_ssm_k, A_ssm_D)
    g = _silu(_B_ref(xn, B_w_gate, B_w_up, B_w_down))
    return x + s * g
```

```cpp
#include <hip/hip_runtime.h>
#include <hip/hip_bf16.h>
typedef unsigned short u16;
static constexpr int S=2048, D=512, F=1792;
__device__ __forceinline__ float b2f(u16 x){ return __uint_as_float(((unsigned)x)<<16); }
__device__ __forceinline__ u16 f2b(float x){ unsigned u=__float_as_uint(x); u += 0x7fffu + ((u>>16)&1u); return (u16)(u>>16); }
__device__ __forceinline__ float rb(float x){ return b2f(f2b(x)); }

__global__ void k_ln(const u16* x, u16* xn){
  int s=blockIdx.x; int t=threadIdx.x;
  float v[8]; float sum=0;
  for(int j=0;j<8;j++){ v[j]=b2f(x[s*D+t*8+j]); sum+=v[j]; }
  for(int m=32;m>=1;m>>=1) sum+=__shfl_xor(sum,m);
  float mean=sum/D; float sq=0;
  for(int j=0;j<8;j++){ float d=v[j]-mean; sq+=d*d; }
  for(int m=32;m>=1;m>>=1) sq+=__shfl_xor(sq,m);
  float var=rb(sq/D); float mb=rb(mean);
  float sd=rb(sqrtf(rb(var+1e-5f)));
  for(int j=0;j<8;j++) xn[s*D+t*8+j]=f2b(rb(v[j]-mb)/sd);
}
__global__ void k_kv(const u16* k,const u16* v,const u16* sk,u16* kvT){
  int i=blockIdx.x*256+threadIdx.x; int s=i/D, d=i%D;
  float a=0;
  for(int t=0;t<4;t++){ int ss=s-3+t; float kk= ss>=0? b2f(k[ss*D+d]):0.f; a=rb(a+rb(kk*b2f(sk[d*4+3-t]))); }
  kvT[d*S+s]=f2b(a*b2f(v[s*D+d]));
}
__global__ void k_conv(const u16* kvT,const u16* h,const u16* Dv,u16* y2){
  __shared__ float su[S], sh[S];
  int d=blockIdx.x;
  for(int i=threadIdx.x;i<S;i+=256){ su[i]=b2f(kvT[d*S+i]); sh[i]=b2f(h[d*S+i]); }
  __syncthreads();
  for(int s=threadIdx.x;s<S;s+=256){ float a=0; for(int j=0;j<=s;j++) a+=sh[j]*su[s-j];
    y2[s*D+d]=f2b(rb(a)+rb(su[s]*b2f(Dv[d]))); }
}
__global__ void k_g1(const u16* xn,const u16* wg,const u16* wu,u16* h){
  __shared__ float sx[D];
  int s=blockIdx.x;
  for(int i=threadIdx.x;i<D;i+=256) sx[i]=b2f(xn[s*D+i]);
  __syncthreads();
  for(int f=threadIdx.x;f<F;f+=256){ float g=0,u=0; for(int k=0;k<D;k++){ g+=sx[k]*b2f(wg[k*F+f]); u+=sx[k]*b2f(wu[k*F+f]); }
    g=rb(g); u=rb(u);
    float ge=rb(0.5f*g*(1.f+tanhf(0.7978845608f*(g+0.044715f*g*g*g))));
    h[s*F+f]=f2b(ge*u); }
}
__global__ void k_g2(const u16* h,const u16* wd,const u16* x,const u16* xn,const u16* y2,u16* out){
  __shared__ float sh[F];
  int s=blockIdx.x;
  for(int i=threadIdx.x;i<F;i+=256) sh[i]=b2f(h[s*F+i]);
  __syncthreads();
  for(int d=threadIdx.x;d<D;d+=256){ float z=0; for(int k=0;k<F;k++) z+=sh[k]*b2f(wd[k*D+d]);
    z=rb(z); float g=rb(z/(1.f+expf(-z)));
    float sv=rb(b2f(xn[s*D+d])*b2f(y2[s*D+d]));
    out[s*D+d]=f2b(b2f(x[s*D+d])+rb(sv*g)); }
}
extern "C" void kernel_launch(void* const* d_in, const int* in_sizes, int n_in, void* d_out, int out_size, void* d_ws, size_t ws_size, hipStream_t stream){
  const u16* q=(const u16*)d_in[0]; const u16* k=(const u16*)d_in[1]; const u16* v=(const u16*)d_in[2];
  const u16* sk=(const u16*)d_in[3]; const u16* ssmk=(const u16*)d_in[4]; const u16* ssmD=(const u16*)d_in[5];
  const u16* wg=(const u16*)d_in[7]; const u16* wu=(const u16*)d_in[8]; const u16* wd=(const u16*)d_in[9];
  u16* ws=(u16*)d_ws; u16* xn=ws; u16* kvT=ws+S*D; u16* y2=ws+2*S*D; u16* h=ws+3*S*D;
  k_ln<<<S,64,0,stream>>>(q,xn);
  k_kv<<<S*D/256,256,0,stream>>>(k,v,sk,kvT);
  k_conv<<<D,256,0,stream>>>(kvT,ssmk,ssmD,y2);
  k_g1<<<S,256,0,stream>>>(xn,wg,wu,h);
  k_g2<<<S,256,0,stream>>>(h,wd,q,xn,y2,(u16*)d_out);
}
```

```cpp
#include <hip/hip_runtime.h>
#include <stdint.h>
#include <utility>

namespace {
typedef unsigned short u16;
typedef unsigned long long u64;
typedef unsigned u32x4 __attribute__((ext_vector_type(4)));
typedef unsigned u32x2 __attribute__((ext_vector_type(2)));
typedef short bf16x8 __attribute__((ext_vector_type(8)));
typedef short s16x4 __attribute__((ext_vector_type(4)));
typedef float f32x2 __attribute__((ext_vector_type(2)));
typedef float f32x4 __attribute__((ext_vector_type(4)));
typedef float f32x16 __attribute__((ext_vector_type(16)));
typedef __bf16 b16x2 __attribute__((ext_vector_type(2)));
typedef __attribute__((address_space(3))) void* lptr_t;
typedef __attribute__((address_space(3))) s16x4* lds_s4;
typedef __attribute__((address_space(1))) u64 gu64;

constexpr int S = 2048, D = 512, F = 1792;
constexpr int WS_EPOCH = 0, WS_HFLAG = 5 << 18, WS_KVFLAG = 20480;
constexpr int WS_STATS = 32768, WS_COLSUM = 3 << 19;
constexpr int WS_KVT = 2 << 20, WS_YT = 4 << 20, WS_H = 8 << 20, WS_END = WS_H + S * F * 2;
constexpr int LDS_BYTES = 163840;
constexpr int SLOT = 49152;
constexpr int L_MISC = 3 * SLOT;
constexpr int L_STATS = L_MISC, L_CS = L_MISC + 1024, L_SYNC = L_MISC + 2048, L_TAG = L_MISC + 2304;
constexpr int L_U = L_MISC + 2688;
constexpr int USTRIDE = 4608;
constexpr int WSLOT = 16384;
constexpr int HKC = 4160;
constexpr int L_HK = 8 * WSLOT;
constexpr int L_HK3 = L_U + 2 * USTRIDE + 64;
constexpr int L_DUMMY = LDS_BYTES - 256;
constexpr int L_Z = 6 * WSLOT;
constexpr int ZROW = 272;
constexpr int L_YL = L_U;
constexpr int YROW = 144;
static_assert(L_HK3 + HKC <= L_DUMMY && L_HK + 3 * HKC <= L_MISC, "lds");
static_assert((L_HK3 - (L_HK + 2 * HKC)) % 128 == 64 && HKC % 128 == 64, "bank offset between the copies of a channel");
static_assert(L_Z + 2 * 64 * ZROW <= L_MISC, "lds");
static_assert(64 * YROW <= 2 * USTRIDE, "lds");
enum { Y_DONE1 = 0, Y_LAND1 = 4, Y_EPI = 6, Y_KV = 8, Y_HSEEN = 10, Y_CVA = 12, Y_CVB = 16, Y_CVD = 20, Y_DONE2 = 24, Y_CS = 28, Y_PATH = 32, Y_LATE = 36 };

constexpr int HFS = 128;
constexpr int SIG_AT = 4, SIG_B = 2;
constexpr int TAG1_AT = 7;
constexpr unsigned SPIN_CAP = 1u << 15;
#define RLX_AGENT __ATOMIC_RELAXED, __HIP_MEMORY_SCOPE_AGENT
#define DI __device__ __forceinline__
#define SB0() __builtin_amdgcn_sched_barrier(0)
#define BAR() asm volatile("s_waitcnt lgkmcnt(0)\n\ts_barrier" ::: "memory")
#define RAWBAR() asm volatile("s_barrier" ::: "memory")
#define PIN_TAG(t) do { t = (((u64)(unsigned)__builtin_amdgcn_readfirstlane((unsigned)(t >> 32)) << 32) | (unsigned)__builtin_amdgcn_readfirstlane((unsigned)t)) + 1; \
    asm volatile("" : "+s"(t)); } while (0)
#define WAITV(n) asm volatile("s_waitcnt vmcnt(" #n ")" ::: "memory")

struct Params {
  const u16 *q, *k, *v, *sk, *ssmk, *ssmd, *wg, *wu, *wd;
  u16* out;
  char* ws;
};

template <class Fn, int... I> DI void static_for_impl(Fn&& f, std::integer_sequence<int, I...>) { (f(std::integral_constant<int, I>{}), ...); }
template <int N, class Fn> DI void static_for(Fn&& f) { static_for_impl(f, std::make_integer_sequence<int, N>{}); }

DI float lo16(unsigned x) { return __uint_as_float(x << 16); }
DI float hi16(unsigned x) { return __uint_as_float(x & 0xffff0000u); }
DI unsigned pk2(float a, float b) { f32x2 t = {a, b}; b16x2 c = __builtin_convertvector(t, b16x2); return __builtin_bit_cast(unsigned, c); }
DI float add1(float a, float b) { float r; asm("v_add_f32 %0, %1, %2" : "=v"(r) : "v"(a), "v"(b)); return r; }
DI float mul1(float a, float b) { float r; asm("v_mul_f32 %0, %1, %2" : "=v"(r) : "v"(a), "v"(b)); return r; }
DI float fma1(float a, float b, float c) { float r; asm("v_fma_f32 %0, %1, %2, %3" : "=v"(r) : "v"(a), "v"(b), "v"(c)); return r; }
DI float rbf(float a) { return lo16(pk2(a, a)); }
DI __amdgpu_buffer_rsrc_t mkrs(const void* p, int bytes) { return __builtin_amdgcn_make_buffer_rsrc((void*)p, 0, bytes, 0x00020000); }
DI void dma16(__amdgpu_buffer_rsrc_t rs, char* lds, int voff, int soff) {
  __builtin_amdgcn_raw_ptr_buffer_load_lds(rs, (lptr_t)lds, 16, voff, soff, 0, 0);
}
template <int CTRL, int RM> DI float dpp_add(float x) {
  return x + __int_as_float(__builtin_amdgcn_update_dpp(0, __float_as_int(x), CTRL, RM, 0xf, false));
}
DI float wave_sum(float x) {
  x = dpp_add<0x111, 0xf>(x); x = dpp_add<0x112, 0xf>(x); x = dpp_add<0x114, 0xf>(x); x = dpp_add<0x118, 0xf>(x);
  x = dpp_add<0x142, 0xa>(x); x = dpp_add<0x143, 0xc>(x);
  return __int_as_float(__builtin_amdgcn_readlane(__float_as_int(x), 63));
}
template <int SLEEP = 2> DI void poll_tags(const char* base, int n, int stride, u64 tag, int lane) {
  for (unsigned spins = 0; spins < SPIN_CAP; ++spins) {
    u64 f = tag;
    if (lane < n) f = __hip_atomic_load((gu64*)base + lane * stride, RLX_AGENT);
    if (__all(f == tag)) break;
    __builtin_amdgcn_s_sleep(SLEEP);
  }
  asm volatile("" ::: "memory");
}
DI u64 read_epoch(const Params& P) { return __hip_atomic_load((const gu64*)(P.ws + WS_EPOCH), RLX_AGENT); }
DI void set_tag(char* p, u64 tag) { __hip_atomic_store((gu64*)p, tag, RLX_AGENT); }
typedef volatile __attribute__((address_space(3))) unsigned* lds_w;
DI lds_w syw(char* smem, int i) { return (lds_w)(smem + L_SYNC) + i; }
DI u64 lds_tag(const char* smem) {
  const u32x2 tw = *(const u32x2*)(smem + L_TAG);
  return ((u64)(unsigned)__builtin_amdgcn_readfirstlane(tw[1]) << 32) | (unsigned)__builtin_amdgcn_readfirstlane(tw[0]);
}
DI void lds_set(char* smem, int i, unsigned v, int lane) {
  asm volatile("" ::: "memory");
  *(lds_w)(smem + (lane ? L_DUMMY + lane * 4 : L_SYNC + i * 4)) = v;
  asm volatile("" ::: "memory");
}
DI u32x2 lds_peek2(char* smem, int i) { return *(volatile __attribute__((address_space(3))) u32x2*)(smem + L_SYNC + i * 4); }
DI u32x4 lds_peek4(char* smem, int i) { return *(volatile __attribute__((address_space(3))) u32x4*)(smem + L_SYNC + i * 4); }
DI unsigned smin(unsigned a, unsigned b) {
  const unsigned x = __builtin_amdgcn_readfirstlane(a), y = __builtin_amdgcn_readfirstlane(b);
  return x < y ? x : y;
}
template <int N> DI unsigned lds_look(char* smem, int i) {
  if (N == 1) return __builtin_amdgcn_readfirstlane(*syw(smem, i));
  if (N == 2) { const u32x2 v = lds_peek2(smem, i); return smin(v[0], v[1]); }
  const u32x4 v = lds_peek4(smem, i);
  return smin(smin(v[0], v[1]), smin(v[2], v[3]));
}
template <int N, int SLEEP = 1> DI void lds_wait(char* smem, int i, unsigned want) {
  for (unsigned spins = 0; lds_look<N>(smem, i) < want && spins < (SPIN_CAP << 4); ++spins)
    if (SLEEP) __builtin_amdgcn_s_sleep(SLEEP);
  asm volatile("" ::: "memory");
}
template <int N, int SLEEP = 1> DI void lds_wait(char* smem, int i, unsigned want, unsigned seen) {
  asm volatile("" : "+s"(seen));
  if (__builtin_expect(seen < want, 0)) lds_wait<N, SLEEP>(smem, i, want);
  asm volatile("" ::: "memory");
}

DI void row_stats(const Params& P, int b, int sw, int lane, u64 tag, const u32x4 (&xr)[4]) {
  float m[4], v[4], a[4][8];
#pragma unroll
  for (int i = 0; i < 4; ++i) {
#pragma unroll
    for (int j = 0; j < 4; ++j) { a[i][2 * j] = lo16(xr[i][j]); a[i][2 * j + 1] = hi16(xr[i][j]); }
    m[i] = add1(add1(add1(a[i][0], a[i][1]), add1(a[i][2], a[i][3])), add1(add1(a[i][4], a[i][5]), add1(a[i][6], a[i][7])));
  }
#pragma unroll
  for (int i = 0; i < 4; ++i) { asm("s_nop 1" : "+v"(m[i])); m[i] = wave_sum(m[i]) * (1.f / D); }
#pragma unroll
  for (int i = 0; i < 4; ++i) {
    v[i] = 0.f;
#pragma unroll
    for (int j = 0; j < 8; ++j) { const float d = add1(a[i][j], -m[i]); v[i] = fma1(d, d, v[i]); }
  }
#pragma unroll
  for (int i = 0; i < 4; ++i) { asm("s_nop 1" : "+v"(v[i])); v[i] = wave_sum(v[i]) * (1.f / D); }
  if (lane < 4) {
    const float mm = lane == 0 ? m[0] : lane == 1 ? m[1] : lane == 2 ? m[2] : m[3];
    const float vv = lane == 0 ? v[0] : lane == 1 ? v[1] : lane == 2 ? v[2] : v[3];
    const unsigned w = pk2(mm, __builtin_amdgcn_sqrtf(rbf(rbf(vv) + 1e-5f)));
    __hip_atomic_store((gu64*)(P.ws + WS_STATS) + b * 8 + sw * 4 + lane, ((u64)(unsigned)tag << 32) | w, RLX_AGENT);
  }
}

struct KvRegs { u32x2 k[2][7], v[2][4]; u32x4 sk[2]; };
DI void kv_load(const Params& P, int b, int sw, int lane, KvRegs& R) {
  const int rq = lane >> 4, c0 = (b & 7) * 64 + (lane & 15) * 4;
#pragma unroll
  for (int h = 0; h < 2; ++h) {
    const int s0 = (b >> 3) * 64 + sw * 32 + h * 16 + rq * 4;
#pragma unroll
    for (int i = 0; i < 7; ++i) { int r = s0 - 3 + i; r = r < 0 ? 0 : r; R.k[h][i] = *(const u32x2*)(P.k + r * D + c0); }
#pragma unroll
    for (int i = 0; i < 4; ++i) R.v[h][i] = *(const u32x2*)(P.v + (s0 + i) * D + c0);
    if (h == 0) { R.sk[0] = *(const u32x4*)(P.sk + c0 * 4); R.sk[1] = *(const u32x4*)(P.sk + c0 * 4 + 8); }
  }
  SB0();
}
DI void kv_half(char* smem, int b, int sw, int lane, const KvRegs& R, int h) {
  const int rq = lane >> 4, cq = lane & 15, u8 = 15 - 8 * sw - 4 * h - rq;
  const unsigned keep = (b < 8 && sw == 0 && rq == 0) ? 0u : ~0u;
  char* tb = smem + L_U + cq * 512 + ((((u8 >> 1) ^ (cq & 7)) << 4) | ((u8 & 1) << 3));
#pragma unroll
  for (int c = 0; c < 4; ++c) {
    const unsigned t01 = R.sk[c >> 1][(c & 1) * 2], t23 = R.sk[c >> 1][(c & 1) * 2 + 1];
    const float w0 = lo16(t01), w1 = hi16(t01), w2 = lo16(t23), w3 = hi16(t23);
    float kk[7], o[4];
#pragma unroll
    for (int i = 0; i < 7; ++i) {
      const unsigned w = (h == 0 && i < 3) ? (R.k[h][i][c >> 1] & keep) : R.k[h][i][c >> 1];
      kk[i] = (c & 1) ? hi16(w) : lo16(w);
    }
#pragma unroll
    for (int r = 0; r < 4; ++r) {
      const float ks = fma1(kk[r + 3], w0, fma1(kk[r + 2], w1, fma1(kk[r + 1], w2, mul1(kk[r], w3))));
      o[r] = mul1(ks, (c & 1) ? hi16(R.v[h][r][c >> 1]) : lo16(R.v[h][r][c >> 1]));
    }
    *(u32x2*)(tb + c * 128) = u32x2{pk2(o[3], o[2]), pk2(o[1], o[0])};
  }
  SB0();
}
DI void kv_store(const Params& P, char* smem, int b, int sw, int lane) {
  lds_set(smem, Y_KV + sw, 1u, lane);
  lds_wait<1>(smem, Y_KV + (sw ^ 1), 1u);
  const __amdgpu_buffer_rsrc_t rws = mkrs(P.ws, WS_END);
#pragma unroll
  for (int i = 0; i < 4; ++i) {
    const int ch = sw * 32 + i * 8 + (lane >> 3);
    const u32x4 t = *(const u32x4*)(smem + L_U + (sw * 32 + i * 8) * 128 + lane * 16);
    __builtin_amdgcn_raw_buffer_store_b128(t, rws, WS_KVT + ((b & 7) * 64 + ch) * 4096 + (1984 - 64 * (b >> 3)) * 2 + (((lane & 7) ^ ((ch >> 2) & 7)) << 4), 0, 16);
  }
  lds_set(smem, Y_KV + sw, 2u, lane);
}
DI void kv_publish(const Params& P, int b, int sw, int lane, u64 tag) {
  asm volatile("s_waitcnt vmcnt(0)" ::: "memory");
  if (lane == 0) set_tag(P.ws + WS_KVFLAG + (((b & 7) * 2 + sw) * 32 + (b >> 3)) * 8, tag);
}

DI u32x4 settle(const Params& P, u32x4 g, int off, u64 tag) {
  const __amdgpu_buffer_rsrc_t rws = mkrs(P.ws, WS_END);
  for (unsigned spins = 0; spins < SPIN_CAP; ++spins) {
    if (__all(g[1] == (unsigned)tag && g[3] == (unsigned)tag)) break;
    __builtin_amdgcn_s_sleep(2);
    g = __builtin_amdgcn_raw_buffer_load_b128(rws, off, 0, 16);
  }
  return g;
}

DI void g1_frags(const char* smem, int slot, int ks, const int (&ax)[2][4], const int (&aw)[3][4], bf16x8 (&xa)[2], bf16x8 (&wa)[4]) {
#pragma unroll
  for (int fb = 0; fb < 4; ++fb) {
    if (fb < 2) xa[fb] = *(const bf16x8*)(smem + (slot & 1) * SLOT + fb * 4096 + ax[slot >> 1][ks]);
    const char* p = smem + ks * 8192 + aw[slot][fb];
    const s16x4 a = __builtin_amdgcn_ds_read_tr16_b64_v4i16((lds_s4)p);
    const s16x4 b = __builtin_amdgcn_ds_read_tr16_b64_v4i16((lds_s4)(p + 2048));
    wa[fb] = __builtin_shufflevector(a, b, 0, 1, 2, 3, 4, 5, 6, 7);
  }
}

DI void g1_compute(const Params& P, char* smem, int p, int c, int wave, int lane) {
  const short one = ((lane & 15) == ((lane >> 4) & 1)) ? (short)0x3f80 : (short)0;
  const bf16x8 mask = {one, one, one, one, one, one, one, one};
  f32x4 csum[2] = {{0.f, 0.f, 0.f, 0.f}, {0.f, 0.f, 0.f, 0.f}};
  const int blk0 = (wave >> 1) * 4 + (wave & 1) * 2;
  const int sh = wave & 1, hh = lane >> 5, r31 = lane & 31;
  const int row = 64 * sh + r31;
  int ax[2][4], aw[3][4], adone = lane ? L_DUMMY + lane * 4 : L_SYNC + (Y_DONE1 + wave) * 4, aland = L_SYNC + Y_LAND1 * 4;
  {
    const int p4 = lane & 3, q = (lane & 15) >> 2, gu = (lane >> 4) & 1;
#pragma unroll
    for (int i = 0; i < 4; ++i) {
#pragma unroll
      for (int z = 0; z < 2; ++z) { ax[z][i] = z * 2 * SLOT + row * 128 + (((2 * i + hh) ^ ((row >> 1) & 7)) << 4); asm volatile("" : "+v"(ax[z][i])); }
#pragma unroll
      for (int z = 0; z < 3; ++z) {
        aw[z][i] = z * SLOT + 16384 + 4096 * hh + 1024 * gu + 256 * q + ((blk0 ^ i ^ (2 * q + gu)) << 5) + 8 * p4;
        asm volatile("" : "+v"(aw[z][i]));
      }
    }
    asm volatile("" : "+v"(adone), "+v"(aland));
  }
  f32x16 acc[4][2];
  f32x2 st[2];
  f32x4 cs[4][4];
  bf16x8 xa[2][2], wa[2][4];
#define G1_PIN() asm volatile("" : "+v"(acc[0][0]), "+v"(acc[0][1]), "+v"(acc[1][0]), "+v"(acc[1][1]), "+v"(acc[2][0]), "+v"(acc[2][1]), "+v"(acc[3][0]), "+v"(acc[3][1]), "+v"(csum[0]), "+v"(csum[1]))
#define G1_MMA(fb, sb) acc[fb][sb] = __builtin_amdgcn_mfma_f32_32x32x16_bf16(wa[cur][fb], xa[cur][sb], acc[fb][sb], 0, 0, 0)
#pragma unroll
  for (int i = 0; i < 4; ++i)
#pragma unroll
    for (int j = 0; j < 2; ++j)
#pragma unroll
      for (int k = 0; k < 16; ++k) acc[i][j][k] = 0.f;
  lds_wait<2, 0>(smem, Y_LAND1, 1u);
  u32x2 pk = {0u, 0u};
  unsigned vdone = 0u;
  g1_frags(smem, 0, 0, ax, aw, xa[0], wa[0]);
#pragma unroll
  for (int t = 0; t < 8; ++t) {
#pragma unroll
    for (int ks = 0; ks < 4; ++ks) {
      const int cur = ks & 1;
      const bool sync = ks & 1, epi = t == 7 && ks >= 2;
      SB0();
      if (!sync) {
        pk = *(volatile __attribute__((address_space(3))) u32x2*)(smem + aland + (epi ? (Y_EPI - Y_LAND1) * 4 : 0));
        g1_frags(smem, t % 3, ks + 1, ax, aw, xa[cur ^ 1], wa[cur ^ 1]);
        if (ks == 2) *(lds_w)(smem + adone) = vdone;
      } else {
        G1_MMA(0, 0);
        SB0();
        const unsigned seen = smin(pk[0], pk[1]), want = epi ? 1 : 2 * t + 1 + (ks >> 1) + 1;
        SB0();
        G1_MMA(0, 1);
        G1_PIN();
        SB0();
        lds_wait<2, 0>(smem, epi ? Y_EPI : Y_LAND1, want, seen);
        SB0();
        if (ks == 1) g1_frags(smem, t % 3, 2, ax, aw, xa[cur ^ 1], wa[cur ^ 1]);
        else if (t < 7) g1_frags(smem, (t + 1) % 3, 0, ax, aw, xa[cur ^ 1], wa[cur ^ 1]);
        else {
#pragma unroll
          for (int sb = 0; sb < 2; ++sb) st[sb] = *(const f32x2*)(smem + L_STATS + (row + 32 * sb) * 8);
#pragma unroll
          for (int fb = 0; fb < 4; ++fb) {
            const float* cp = (const float*)(smem + L_CS) + (blk0 ^ fb) * 16 + 4 * hh;
            cs[fb][0] = *(const f32x4*)cp; cs[fb][1] = *(const f32x4*)(cp + 8); cs[fb][2] = *(const f32x4*)(cp + 128); cs[fb][3] = *(const f32x4*)(cp + 136);
          }
        }
      }
#pragma unroll
      for (int fb = (sync ? 1 : 0); fb < 4; ++fb) { G1_MMA(fb, 0); G1_MMA(fb, 1); }
      if (t < 2) {
#pragma unroll
        for (int fb = 0; fb < 2; ++fb) csum[fb] = __builtin_amdgcn_mfma_f32_16x16x32_bf16(wa[cur][fb], mask, csum[fb], 0, 0, 0);
      }
      if (ks == 3 && t == 7) {
#pragma unroll
        for (int g = 0; g < 6; ++g) {
          __builtin_amdgcn_sched_group_barrier(0x008, 1, 0);
          __builtin_amdgcn_sched_group_barrier(0x100, 3, 0);
        }
      } else {
#pragma unroll
        for (int g = 0; g < 5; ++g) {
          __builtin_amdgcn_sched_group_barrier(0x008, 1, 0);
          __builtin_amdgcn_sched_group_barrier(0x100, 2, 0);
        }
        if (ks == 2) {
          __builtin_amdgcn_sched_group_barrier(0x008, 1, 0);
          __builtin_amdgcn_sched_group_barrier(0x080, 2, 0);
        }
        if (ks == 0) {
          __builtin_amdgcn_sched_group_barrier(0x008, 1, 0);
          __builtin_amdgcn_sched_group_barrier(0x080, 1, 0);
        }
        __builtin_amdgcn_sched_group_barrier(0x008, 5, 0);
      }
      if (ks == 0) { ++vdone; asm volatile("" : "+v"(vdone)); }
      if (t == 2 && ks == 0) G1_PIN();
      if (t == 2 && ks == 0 && !(p & 3)) {
        SB0();
        if ((lane & 15) < 2) {
#pragma unroll
          for (int fb = 0; fb < 2; ++fb) *(f32x4*)(smem + L_CS + (lane & 1) * 512 + ((blk0 ^ fb) * 16 + (lane >> 4) * 4) * 4) = csum[fb];
        }
        lds_set(smem, Y_CS + wave, 1u, lane);
      }
    }
  }
#undef G1_MMA
#undef G1_PIN
  SB0();
  const __amdgpu_buffer_rsrc_t rws = mkrs(P.ws, WS_END);
  constexpr float C1 = -2.302208198f, C2 = -0.1029432397f;
  float a1[2], a3[2], v2[2];
#pragma unroll
  for (int sb = 0; sb < 2; ++sb) {
    const float r = __builtin_amdgcn_rcpf(st[sb][1]);
    a1[sb] = C1 * r; a3[sb] = C2 * r * r * r; v2[sb] = st[sb][1] * st[sb][1];
  }
  const int hoff = WS_H + ((p * 4 + sh * 2) * 112 + c * 8) * 1024;
  SB0();
#pragma unroll
  for (int fb = 0; fb < 4; ++fb) {
    const f32x4 g0 = cs[fb][0], g1 = cs[fb][1], u0 = cs[fb][2], u1 = cs[fb][3];
    const f32x2 cg[4] = {{g0[0], g0[1]}, {g0[2], g0[3]}, {g1[0], g1[1]}, {g1[2], g1[3]}};
    const f32x2 cu[4] = {{u0[0], u0[1]}, {u0[2], u0[3]}, {u1[0], u1[1]}, {u1[2], u1[3]}};
#pragma unroll
    for (int sb = 0; sb < 2; ++sb) {
      if (fb * 2 + sb == TAG1_AT) {
        asm volatile("s_waitcnt vmcnt(%0)" :: "i"(TAG1_AT - 4) : "memory");
        if (lane == 0) set_tag(P.ws + WS_HFLAG + ((p * 2 + sh) * 64 + c * 2 + (wave >> 1)) * HFS, lds_tag(smem));
        SB0();
      }
      const f32x2 nm = {-st[sb][0], -st[sb][0]}, b1 = {a1[sb], a1[sb]}, b3 = {a3[sb], a3[sb]}, sd2 = {v2[sb], v2[sb]};
      u32x4 o;
      f32x2 g[4], tu[4], z[4];
#pragma unroll
      for (int j = 0; j < 4; ++j) g[j] = __builtin_elementwise_fma(nm, cg[j], f32x2{acc[fb][sb][2 * j], acc[fb][sb][2 * j + 1]});
      SB0();
#pragma unroll
      for (int j = 0; j < 4; ++j) z[j] = g[j] * g[j];
      SB0();
#pragma unroll
      for (int j = 0; j < 4; ++j) z[j] = __builtin_elementwise_fma(z[j], b3, b1);
      SB0();
#pragma unroll
      for (int j = 0; j < 4; ++j) z[j] *= g[j];
      SB0();
#pragma unroll
      for (int j = 0; j < 4; ++j) tu[j] = __builtin_elementwise_fma(nm, cu[j], f32x2{acc[fb][sb][8 + 2 * j], acc[fb][sb][9 + 2 * j]});
      SB0();
#pragma unroll
      for (int j = 0; j < 4; ++j) z[j] = f32x2{__builtin_amdgcn_exp2f(z[j][0]), __builtin_amdgcn_exp2f(z[j][1])};
      SB0();
#pragma unroll
      for (int j = 0; j < 4; ++j) tu[j] *= g[j];
      SB0();
#pragma unroll
      for (int j = 0; j < 4; ++j) z[j] = __builtin_elementwise_fma(z[j], sd2, sd2);
      SB0();
#pragma unroll
      for (int j = 0; j < 4; ++j) z[j] = f32x2{__builtin_amdgcn_rcpf(z[j][0]), __builtin_amdgcn_rcpf(z[j][1])};
      SB0();
#pragma unroll
      for (int j = 0; j < 4; ++j) tu[j] *= z[j];
      SB0();
#pragma unroll
      for (int j = 0; j < 4; ++j) o[j] = pk2(tu[j][0], tu[j][1]);
      __builtin_amdgcn_raw_buffer_store_b128(o, rws, hoff + lane * 16 + (sb * 112 + (blk0 ^ fb)) * 1024, 0, 16);
    }
  }
}

#define LATE_WAIT(wl) asm volatile("s_waitcnt vmcnt(0)" : "+v"(wl[0]), "+v"(wl[1]), "+v"(wl[2]), "+v"(wl[3]), "+v"(wl[4]), "+v"(wl[5]), \
    "+v"(wl[6]), "+v"(wl[7]), "+v"(wl[8]), "+v"(wl[9]), "+v"(wl[10]), "+v"(wl[11]), "+v"(wl[12]), "+v"(wl[13]), "+v"(wl[14]), "+v"(wl[15]) :: "memory")
DI void wd_stage(__amdgpu_buffer_rsrc_t rwd, char* smem, int j, int d, int voff, int co) {
  const int so = (d * 896 + (j < 7 ? j : j - 7) * 128) * 1024 + (j < 7 ? co : co ^ 32768);
#pragma unroll
  for (int i = 0; i < 8; ++i) dma16(rwd, smem + (j & 7) * WSLOT + (d * 8 + i) * 1024, voff, so + ((i >> 2) * 64 + (i & 3) * 8) * 1024);
}
DI int wd_voff(int lane) { return (lane >> 3) * 1024 + (((lane & 7) ^ (((lane >> 4) & 1) << 2)) << 4); }

DI void cs_publish(const Params& P, char* smem, int p, int c, int sw, int lane, u64 tag) {
  if (p & 3) return;
  lds_wait<4>(smem, Y_CS, 1u);
  const u32x2 v = *(const u32x2*)(smem + L_CS + sw * 512 + lane * 8);
  const u32x4 o = {v[0], (unsigned)tag, v[1], (unsigned)tag};
  __builtin_amdgcn_raw_buffer_store_b128(o, mkrs(P.ws, WS_END), WS_COLSUM + ((c * 4 + (p >> 2)) * 2 + sw) * 1024 + lane * 16, 0, 16);
}
struct EpiRegs { u32x4 g[4], st; };
DI void epi_load(const Params& P, int p, int c, int sw, int lane, EpiRegs& E) {
  const __amdgpu_buffer_rsrc_t rws = mkrs(P.ws, WS_END);
  E.st = __builtin_amdgcn_raw_buffer_load_b128(rws, WS_STATS + p * 1024 + lane * 16, 0, 16);
#pragma unroll
  for (int i = 0; i < 4; ++i) E.g[i] = __builtin_amdgcn_raw_buffer_load_b128(rws, WS_COLSUM + (c * 8 + sw) * 1024 + lane * 16, i * 2048, 16);
  SB0();
}
DI void epi_tables(const Params& P, char* smem, int p, int c, int sw, int lane, u64 tag, EpiRegs& E) {
  for (unsigned spins = 0; spins < SPIN_CAP; ++spins) {
    bool ok = E.st[1] == (unsigned)tag && E.st[3] == (unsigned)tag;
#pragma unroll
    for (int i = 0; i < 4; ++i) ok = ok && E.g[i][1] == (unsigned)tag && E.g[i][3] == (unsigned)tag;
    if (__all(ok)) break;
    __builtin_amdgcn_s_sleep(8);
    epi_load(P, p, c, sw, lane, E);
  }
  *(f32x2*)(smem + L_CS + sw * 512 + lane * 8) =
      f32x2{add1(add1(__uint_as_float(E.g[0][0]), __uint_as_float(E.g[1][0])), add1(__uint_as_float(E.g[2][0]), __uint_as_float(E.g[3][0]))),
            add1(add1(__uint_as_float(E.g[0][2]), __uint_as_float(E.g[1][2])), add1(__uint_as_float(E.g[2][2]), __uint_as_float(E.g[3][2])))};
  if (sw == 0) *(f32x4*)(smem + L_STATS + lane * 16) = f32x4{lo16(E.st[0]), hi16(E.st[0]), lo16(E.st[2]), hi16(E.st[2])};
  lds_set(smem, Y_EPI + sw, 1u, lane);
}

DI constexpr int hk_base(int copy) { return copy < 3 ? L_HK + copy * HKC : L_HK3; }

DI void g1_dma(const Params& P, char* smem, int p, int c, int rot, int lw, int lane, int co, u32x4 (&wl)[16]) {
  const __amdgpu_buffer_rsrc_t rx = mkrs(P.q, S * D * 2), rg = mkrs(P.wg, D * F * 2), ru = mkrs(P.wu, D * F * 2);
  const __amdgpu_buffer_rsrc_t rwd = mkrs(P.wd, D * F * 2);
  int vx[2], vw[2];
#pragma unroll
  for (int i = 0; i < 2; ++i) {
    vx[i] = (lane >> 3) * 1024 + (((lane & 7) ^ (4 * i + (lane >> 4))) << 4);
    const int q = lane >> 4;
    vw[i] = q * (F * 2) + ((((lane & 15) >> 1) ^ (2 * q + i)) << 5) + (lane & 1) * 16;
  }
  const int vwd = wd_voff(lane);
  const int sx0 = (p * 128 + lw * 64) * 1024, sw0 = lw * 4 * (F * 2) + c * 256;
  u32x4 r35[24];
  const int v35 = vwd + lw * 896 * 1024 + co, vlate = vwd + lw * 64 * 1024 + (co ^ 32768);
#define LD8(dst, base, off) _Pragma("unroll") for (int i = 0; i < 8; ++i) \
    asm volatile("s_nop 4\n\tglobal_load_dwordx4 %0, %1, %2" : "=v"(dst) : "v"(base + (off) * 1024), "s"(P.wd) : "memory")
  int slot = 0;
  auto stage = [&](int t, auto T_) __attribute__((always_inline)) {
    constexpr int T = decltype(T_)::value;
    const int k = (t + rot) & 7;
    const int sx = sx0 + k * 128, sw = sw0 + k * 64 * (F * 2);
    char* lx = smem + slot + lw * 8192;
    char* lwp = smem + slot + 16384 + lw * 2048;
    if (t > 2) lds_wait<4, 1>(smem, Y_DONE1, t - 2);
#pragma unroll
    for (int i = 0; i < 8; ++i) {
      if (i == SIG_AT && t) {
        if (T > 3) asm volatile("s_waitcnt vmcnt(%0)" :: "i"(16 + SIG_AT) : "memory");
        else asm volatile("s_waitcnt vmcnt(%0)" :: "i"(8 + SIG_AT) : "memory");
        lds_set(smem, Y_LAND1 + lw, 2 * t - 1, lane);
      }
      dma16(rx, lx + i * 1024, vx[i & 1], sx + i * 8192);
    }
#pragma unroll
    for (int i = 0; i < 8; ++i) {
      if (i == SIG_B && t) {
        if (T > 3) asm volatile("s_waitcnt vmcnt(%0)" :: "i"(16 + 2 * SIG_B) : "memory");
        else asm volatile("s_waitcnt vmcnt(%0)" :: "i"(8 + 2 * SIG_B) : "memory");
        lds_set(smem, Y_LAND1 + lw, 2 * t, lane);
      }
      dma16(rg, lwp + i * 4096, vw[0], sw + i * 8 * (F * 2));
      dma16(ru, lwp + i * 4096 + 1024, vw[1], sw + i * 8 * (F * 2));
    }
    if (!t) RAWBAR();
    if constexpr (T == 3) { LD8(wl[i], vlate, (1 + (i >> 2)) * 128 + (i & 3) * 8); }
    if constexpr (T == 4) { LD8(wl[8 + i], vlate, (3 + (i >> 2)) * 128 + (i & 3) * 8); }
    if constexpr (T == 5) { LD8(r35[i], v35, 3 * 128 + (i >> 2) * 64 + (i & 3) * 8); }
    if constexpr (T == 6) { LD8(r35[8 + i], v35, 4 * 128 + (i >> 2) * 64 + (i & 3) * 8); }
    if constexpr (T == 7) { LD8(r35[16 + i], v35, 5 * 128 + (i >> 2) * 64 + (i & 3) * 8); }
    slot = slot == 2 * SLOT ? 0 : slot + SLOT;
  };
#pragma unroll 1
  for (int t = 0; t < 3; ++t) stage(t, std::integral_constant<int, 0>{});
  static_for<5>([&](auto I_) __attribute__((always_inline)) { stage(3 + decltype(I_)::value, std::integral_constant<int, 3 + decltype(I_)::value>{}); });
#undef LD8
  WAITV(16); lds_set(smem, Y_LAND1 + lw, 15u, lane);
  WAITV(8); lds_set(smem, Y_LAND1 + lw, 16u, lane);
  lds_wait<4>(smem, Y_DONE1, 6u);
  wd_stage(rwd, smem, 6, lw, vwd, co); wd_stage(rwd, smem, 7, lw, vwd, co);
  lds_wait<4>(smem, Y_DONE1, 7u);
  wd_stage(rwd, smem, 0, lw, vwd, co); wd_stage(rwd, smem, 1, lw, vwd, co); wd_stage(rwd, smem, 2, lw, vwd, co);
  lds_wait<4>(smem, Y_DONE1, 8u);
  asm volatile("s_waitcnt vmcnt(40)" : "+v"(r35[0]), "+v"(r35[1]), "+v"(r35[2]), "+v"(r35[3]), "+v"(r35[4]), "+v"(r35[5]), "+v"(r35[6]), "+v"(r35[7]),
      "+v"(r35[8]), "+v"(r35[9]), "+v"(r35[10]), "+v"(r35[11]) :: "memory");
  asm volatile("" : "+v"(r35[12]), "+v"(r35[13]), "+v"(r35[14]), "+v"(r35[15]), "+v"(r35[16]), "+v"(r35[17]), "+v"(r35[18]), "+v"(r35[19]),
      "+v"(r35[20]), "+v"(r35[21]), "+v"(r35[22]), "+v"(r35[23]) :: "memory");
#pragma unroll
  for (int i = 0; i < 24; ++i) *(u32x4*)(smem + (3 + (i >> 3)) * WSLOT + (lw * 8 + (i & 7)) * 1024 + lane * 16) = r35[i];
  LATE_WAIT(wl); lds_set(smem, Y_LAND1 + lw, 17u, lane);
}

DI void conv_stage(const Params& P, char* smem, int d0, int sw, int lane, u64 tag, unsigned h0, unsigned hl) {
  const __amdgpu_buffer_rsrc_t rh = mkrs(P.ssmk, D * S * 2), rws = mkrs(P.ws, WS_END);
  const int ch = d0 + sw;
  char* hb0 = smem + (sw ? hk_base(2) : hk_base(0));
  char* hb1 = smem + (sw ? hk_base(3) : hk_base(1));
#pragma unroll
  for (int i = 0; i < 4; ++i) {
    dma16(rh, hb0 + 32 + i * 1024, lane * 16, ch * 4096 + i * 1024);
    dma16(rh, hb1 + 32 + i * 1024, lane * 16 + 2, ch * 4096 + i * 1024);
  }
  poll_tags<16>(P.ws + WS_KVFLAG + (d0 >> 5) * 256, 32, 1, tag, lane);
  u32x4 t[4];
#pragma unroll
  for (int i = 0; i < 4; ++i) t[i] = __builtin_amdgcn_raw_buffer_load_b128(rws, WS_KVT + ch * 4096 + i * 1024 + lane * 16, 0, 16);
  asm volatile("s_waitcnt vmcnt(4)" ::: "memory");
  if (lane < 8) *(unsigned*)(hb0 + lane * 4) = 0u;
  else if (lane < 16) *(unsigned*)(hb1 + (lane - 8) * 4) = lane == 15 ? (h0 << 16) : 0u;
  else if (lane == 16) *(u16*)(hb1 + 2062 * 2) = (u16)hl;
  lds_wait<2>(smem, Y_KV, 2u);
  char* ub = smem + L_U + sw * USTRIDE;
  if (lane < 32) *(u32x4*)(ub + 4096 + lane * 16) = u32x4{0u, 0u, 0u, 0u};
#pragma unroll
  for (int i = 0; i < 4; ++i) *(u32x4*)(ub + i * 1024 + lane * 16) = t[i];
}

DI void side_stage(const Params& P, char* smem, int d0, int sw, int lane, bool extra) {
  unsigned h0 = P.ssmk[(d0 + sw) * S], hl = P.ssmk[(d0 + sw) * S + S - 1];
  if (!extra) lds_wait<4>(smem, Y_DONE1, 6u);
  asm volatile("" : "+v"(h0), "+v"(hl));
  lds_wait<1>(smem, Y_KV, 1u);
  conv_stage(P, smem, d0, sw, lane, lds_tag(smem), h0, hl);
  lds_set(smem, Y_KV + sw, 3u, lane);
}

DI void conv_phase(const Params& P, char* smem, int d0, int lw, int lane) {
  const int cl = lw >> 1, half = lw & 1, ch = d0 + cl;
  const int m = lane & 15, g = lane >> 4;
  const unsigned lbase = (unsigned)(size_t)(lptr_t)smem;
  const int cpy = cl * 2 + ((m & 1) ^ 1);
  const unsigned aA = lbase + (cpy < 3 ? L_HK + cpy * HKC : L_HK3) + (12 + ((m + 1) >> 1) - 4 * g) * 4 + half * 256;
  const unsigned aB = lbase + L_U + cl * USTRIDE + (2056 - 1792 - 16 * m - 8 * g) * 2 + half * 256;
  const float dv = lo16(P.ssmd[ch]);
  unsigned aAa[8];
#pragma unroll
  for (int a = 0; a < 8; ++a) aAa[a] = aA + a * 512;
  f32x4 acc[8];
#pragma unroll
  for (int t = 0; t < 8; ++t) acc[t] = f32x4{0.f, 0.f, 0.f, 0.f};
  u32x2 A0[2][8], A1[2][8];
  u32x4 B[2][8];
#define RD(buf, z, bb) do { \
    if ((z) % 3 == 0) asm volatile("ds_read_b128 %0, %1 offset:%2" : "=v"(B[buf][(z) / 3]) : "v"(aB), "i"((7 - (z) / 3) * 512 + 64 * (bb)) : "memory"); \
    else if ((z) % 3 == 1) asm volatile("ds_read2_b32 %0, %1 offset0:%2 offset1:%3" : "=v"(A0[buf][(z) / 3]) : "v"(aAa[(z) / 3]), "i"(16 * (bb)), "i"(16 * (bb) + 1) : "memory"); \
    else asm volatile("ds_read2_b32 %0, %1 offset0:%2 offset1:%3" : "=v"(A1[buf][(z) / 3]) : "v"(aAa[(z) / 3]), "i"(16 * (bb) + 2), "i"(16 * (bb) + 3) : "memory"); \
  } while (0)
  SB0();
  __builtin_amdgcn_s_setprio(0);
  lds_wait<1, 2>(smem, Y_KV + cl, 3u);
#pragma unroll
  for (int z = 0; z < 24; ++z) RD(0, z, 0);
  lds_wait<4, 2>(smem, Y_DONE1, 10u);
  __builtin_amdgcn_s_setprio(3);
#pragma unroll
  for (int bb = 0; bb < 4; ++bb) {
    const int cur = bb & 1;
    asm volatile("s_waitcnt lgkmcnt(0)" ::: "memory");
    SB0();
    int n = 0;
#pragma unroll
    for (int dl = 0; dl < 8; ++dl)
#pragma unroll
      for (int a = 0; a + dl < 8; ++a) {
        const u32x4 av = {A0[cur][a][0], A0[cur][a][1], A1[cur][a][0], A1[cur][a][1]};
        acc[a + dl] = __builtin_amdgcn_mfma_f32_16x16x32_bf16(__builtin_bit_cast(bf16x8, av), __builtin_bit_cast(bf16x8, B[cur][dl]), acc[a + dl], 0, 0, 0);
        SB0();
        if (n < 24) {
          if (bb == 0) RD(1, n, 1);
          if (bb == 1) RD(0, n, 2);
          if (bb == 2) RD(1, n, 3);
          SB0();
        }
        ++n;
      }
  }
#undef RD

  char* mine = smem + (lw < 3 ? L_HK + lw * HKC : L_HK3);
  char* other = smem + ((lw ^ 1) < 3 ? L_HK + (lw ^ 1) * HKC : L_HK3);
  if (lane == 0) *syw(smem, Y_CVA + lw) = 2u;
  f32x4 keep[4], snd[4];
#pragma unroll
  for (int i = 0; i < 4; ++i)
#pragma unroll
    for (int j = 0; j < 4; ++j) { const float lo = acc[i][j], hi = acc[4 + i][j]; snd[i][j] = half ? lo : hi; keep[i][j] = half ? hi : lo; }
  lds_wait<1, 0>(smem, Y_CVA + (lw ^ 1), 2u);
#pragma unroll
  for (int i = 0; i < 4; ++i) *(f32x4*)(mine + i * 1024 + lane * 16) = snd[i];
  lds_set(smem, Y_CVB + lw, 2u, lane);
  lds_wait<1, 0>(smem, Y_CVB + (lw ^ 1), 2u);
  const unsigned tg = (unsigned)lds_tag(smem);
  const __amdgpu_buffer_rsrc_t rws = mkrs(P.ws, WS_END);
#pragma unroll
  for (int i = 0; i < 4; ++i) {
    const int t = 4 * half + i;
    const f32x4 o = *(const f32x4*)(other + i * 1024 + lane * 16);
    const u32x2 uu = *(const u32x2*)(smem + L_U + cl * USTRIDE + (2044 - 256 * t - 16 * m - 4 * g) * 2);
    const unsigned c0 = pk2(add1(keep[i][0], o[0]), add1(keep[i][1], o[1])), c1 = pk2(add1(keep[i][2], o[2]), add1(keep[i][3], o[3]));
    const unsigned g0 = pk2(mul1(hi16(uu[1]), dv), mul1(lo16(uu[1]), dv)), g1 = pk2(mul1(hi16(uu[0]), dv), mul1(lo16(uu[0]), dv));
    const u32x4 w = {pk2(add1(lo16(c0), lo16(g0)), add1(hi16(c0), hi16(g0))), tg, pk2(add1(lo16(c1), lo16(g1)), add1(hi16(c1), hi16(g1))), tg};
    __builtin_amdgcn_raw_buffer_store_b128(w, rws, WS_YT + ch * 8192 + (256 * t + 16 * m + 4 * g) * 4, 0, 16);
  }
}

DI void g2_frags(const char* smem, int j, int ks, const int (&aw)[4], bf16x8 (&wa)[2]) {
#pragma unroll
  for (int db = 0; db < 2; ++db) {
    const char* p = smem + (j & 3) * WSLOT + ks * 2048 + aw[(j & 4 ? 2 : 0) + db];
    const s16x4 a = __builtin_amdgcn_ds_read_tr16_b64_v4i16((lds_s4)p);
    const s16x4 b = __builtin_amdgcn_ds_read_tr16_b64_v4i16((lds_s4)(p + 1024));
    wa[db] = __builtin_shufflevector(a, b, 0, 1, 2, 3, 4, 5, 6, 7);
  }
}

constexpr int HT = 4, HD = 10;

struct XRegs { u32x4 x, g; };
DI void x_load(const Params& P, int rt, int ct, int tid, XRegs& X) {
  X.x = *(const u32x4*)(P.q + (rt * 64 + (tid >> 3)) * D + ct * 64 + (tid & 7) * 8);
  X.g = __builtin_amdgcn_raw_buffer_load_b128(mkrs(P.ws, WS_END), WS_STATS + (rt * 64 + ((tid >> 3) & ~1)) * 8, 0, 16);
}
DI unsigned x_settle(const Params& P, int rt, int tid, const XRegs& X, u64 tag) {
  const u32x4 g = settle(P, X.g, WS_STATS + (rt * 64 + ((tid >> 3) & ~1)) * 8, tag);
  return (tid & 8) ? g[2] : g[0];
}

DI void late_load(const Params& P, int co, int lw, int lane, u32x4 (&wl)[16]) {
  const int vo = wd_voff(lane) + ((lw >> 1) * 896 + (lw & 1) * 64) * 1024 + (co ^ 32768);
#pragma unroll
  for (int i = 0; i < 16; ++i) {
    asm volatile("s_nop 4\n\tglobal_load_dwordx4 %0, %1, %2" : "=v"(wl[i]) : "v"(vo + ((1 + (i >> 2)) * 128 + (i & 3) * 8) * 1024), "s"(P.wd) : "memory");
  }
}
DI void late_store(const Params& P, char* smem, int co, int lw, int lane, const u32x4 (&wl)[16]) {
  const __amdgpu_buffer_rsrc_t rwd = mkrs(P.wd, D * F * 2);
  char* dst = smem + lw * 4096;
  lds_wait<2, 2>(smem, Y_DONE2 + (lw & 2), 1u);
#pragma unroll
  for (int i = 0; i < 12; ++i) *(u32x4*)(dst + (i >> 2) * WSLOT + (i & 3) * 1024 + lane * 16) = wl[i];
  lds_set(smem, Y_LATE + lw, 1u, lane);
  lds_wait<2, 2>(smem, Y_DONE2 + (lw & 2), 2u);
  const int voff = wd_voff(lane), so = ((lw >> 1) * 896 + (lw & 1) * 64) * 1024 + (co ^ 32768);
#pragma unroll
  for (int i = 0; i < 8; ++i) dma16(rwd, dst + (4 + (i >> 2)) * WSLOT + (i & 3) * 1024, voff, so + ((5 + (i >> 2)) * 128 + (i & 3) * 8) * 1024);
#pragma unroll
  for (int i = 12; i < 16; ++i) *(u32x4*)(dst + 3 * WSLOT + (i & 3) * 1024 + lane * 16) = wl[i];
  WAITV(0);
  lds_set(smem, Y_LATE + lw, 2u, lane);
}

DI void g2_compute(const Params& P, char* smem, int rt, int ct, int wave, int lane, int tid, u64 tag, XRegs& X) {
  const char* tag2 = P.ws + WS_HFLAG + (rt * 64 + 32) * HFS;
  u32x2 f2 = {0u, 0u};
  const int sb = wave & 1, kh = wave >> 1, hh = lane >> 5;
  const __amdgpu_buffer_rsrc_t rws = mkrs(P.ws, WS_END);
  const int hoff = WS_H + ((rt * 2 + sb) * 112 + kh * 56) * 1024 + (rt & 1) * 2048;
#define HOFF(i) (((i) < 28 ? hoff : hoff ^ 2048) + ((((i) % 28) >> 1) * 4 + ((i) & 1)) * 1024)
  u32x4 hv[HD + 1], ht[HT];
#pragma unroll
  for (int i = 0; i < HD; ++i) hv[i] = __builtin_amdgcn_raw_buffer_load_b128(rws, lane * 16, HOFF(i), 16);
  x_load(P, rt, ct, tid, X);
  int aw[4];
  {
    const int p4 = lane & 3, q = (lane & 15) >> 2, gm = (lane >> 4) & 1;
#pragma unroll
    for (int db = 0; db < 2; ++db) {
      aw[db] = 8192 * kh + 128 * (4 * hh + q) + (((2 * db + gm) ^ (q & 2)) << 5) + 8 * p4;
      aw[2 + db] = aw[db] + 4 * WSLOT;
      asm volatile("" : "+v"(aw[2 + db]));
    }
  }
  f32x16 acc[2];
#pragma unroll
  for (int j = 0; j < 2; ++j)
#pragma unroll
    for (int k = 0; k < 16; ++k) acc[j][k] = 0.f;
  bf16x8 wa[3][2];
  unsigned pl = 0u;
  u32x4 pk4 = {0u, 0u, 0u, 0u};
  u32x2 pk = {0u, 0u};
  int zo = L_Z + (kh * 64 + sb * 32 + (lane & 31)) * ZROW + hh * 16;
  asm volatile("" : "+v"(zo));
  char* zb = smem + zo;
  lds_wait<2>(smem, Y_LAND1, 17u);
  lds_wait<4, 0>(smem, Y_DONE1, 10u);
  g2_frags(smem, 0, 0, aw, wa[0]);
  g2_frags(smem, 0, 1, aw, wa[1]);
  g2_frags(smem, 0, 2, aw, wa[2]);
  constexpr int TAG2_AT = 12, L2 = 24, L3 = 2 * L2 - (28 - HD), NV = 56 - HT, T0 = NV - HD - HT;
  static_for<56>([&](auto I_) __attribute__((always_inline)) {
    constexpr int i = decltype(I_)::value;
    asm volatile("" : "+v"(acc[0]), "+v"(acc[1]));
    SB0();
    if (i == 9 || i == 21 || i == NV - HD) { lds_set(smem, Y_DONE2 + wave, i == 9 ? 1u : i == 21 ? 2u : 3u, lane); SB0(); }
    if (i == 29 || i == 41) { lds_wait<2, 0>(smem, Y_LATE + 2 * kh, i == 29 ? 1u : 2u, smin(pk[0], pk[1])); SB0(); }
    if (i == L2) { lds_wait<1, 1>(smem, Y_HSEEN + 1, 1u, (unsigned)__builtin_amdgcn_readfirstlane(pl)); SB0(); }
    if (i == TAG2_AT) {
      asm volatile("s_nop 4\n\tglobal_load_dwordx2 %0, %1, %2 sc1" : "=v"(f2) : "v"(wave ? 31 * HFS : lane < 28 ? lane * HFS : 0), "s"(tag2) : "memory");
      SB0();
    }
    if (i == L2 - 3) {
      asm volatile("s_waitcnt vmcnt(%1)" : "+v"(f2) : "i"((28 - HD < L2 - 3 ? 28 - HD : L2 - 3) - TAG2_AT) : "memory");
      if (wave == 0) {
        if (!__all(f2[0] == (unsigned)tag && f2[1] == (unsigned)(tag >> 32))) poll_tags<2>(tag2, 28, HFS / 8, tag, lane);
        lds_set(smem, Y_HSEEN + 1, 1u, lane);
      }
      SB0();
    }
    if (i == L2 - 1) pl = *syw(smem, Y_HSEEN + 1);
    if (i == 28 || i == 40) pk = lds_peek2(smem, Y_LATE + 2 * kh);
    if (i == 52) pk4 = lds_peek4(smem, Y_DONE2);
    SB0();
    const bf16x8 hb = __builtin_bit_cast(bf16x8, i < NV ? hv[i % (HD + 1)] : ht[i < NV ? 0 : i - NV]);
    acc[0] = __builtin_amdgcn_mfma_f32_32x32x16_bf16(wa[i % 3][0], hb, acc[0], 0, 0, 0);
    SB0();
    if constexpr (i + HD < 28 || (i >= L3 && i + HD < NV)) hv[(i + HD) % (HD + 1)] = __builtin_amdgcn_raw_buffer_load_b128(rws, lane * 16, HOFF(i + HD), 16);
    if constexpr (i >= L2 && i < L3) hv[(28 + 2 * (i - L2)) % (HD + 1)] = __builtin_amdgcn_raw_buffer_load_b128(rws, lane * 16, HOFF(28 + 2 * (i - L2)), 16);
    SB0();
    acc[1] = __builtin_amdgcn_mfma_f32_32x32x16_bf16(wa[i % 3][1], hb, acc[1], 0, 0, 0);
    SB0();
    if constexpr (i >= L2 && i < L3) hv[(29 + 2 * (i - L2)) % (HD + 1)] = __builtin_amdgcn_raw_buffer_load_b128(rws, lane * 16, HOFF(29 + 2 * (i - L2)), 16);
    if constexpr (i >= T0 && i < T0 + HT) ht[i - T0] = __builtin_amdgcn_raw_buffer_load_b128(rws, lane * 16, HOFF(NV + i - T0), 16);
    if (i < 53) g2_frags(smem, (i + 3) >> 2, (i + 3) & 3, aw, wa[i % 3]);
  });
  static_assert(NV - HD >= 30 && T0 >= L3 && L2 >= 28 - HD, "progress 3 follows the reads of stage 7; early requests follow the second tags");
  SB0();
  lds_wait<4>(smem, Y_DONE2, 3u, smin(smin(pk4[0], pk4[1]), smin(pk4[2], pk4[3])));
  if (kh) lds_wait<2>(smem, Y_CVD, 1u);
#pragma unroll
  for (int db = 0; db < 2; ++db)
#pragma unroll
    for (int i = 0; i < 4; ++i)
      *(f32x4*)(zb + db * 128 + i * 32) = f32x4{acc[db][4 * i], acc[db][4 * i + 1], acc[db][4 * i + 2], acc[db][4 * i + 3]};
}

DI void y_tile(const Params& P, char* smem, int rt, int ct, int lw, int lane, u64 tag) {
  const __amdgpu_buffer_rsrc_t rws = mkrs(P.ws, WS_END);
  lds_wait<4, 4>(smem, Y_DONE2, 3u);
  const int off = WS_YT + (ct * 64 + lw * 16 + (lane >> 4)) * 8192 + rt * 256 + (lane & 15) * 16;
  u32x4 yv[4];
  for (unsigned spins = 0; spins < SPIN_CAP; ++spins) {
    bool ok = true;
#pragma unroll
    for (int i = 0; i < 4; ++i) yv[i] = __builtin_amdgcn_raw_buffer_load_b128(rws, off, i * 32768, 16);
#pragma unroll
    for (int i = 0; i < 4; ++i) ok = ok && yv[i][1] == (unsigned)tag && yv[i][3] == (unsigned)tag;
    if (__all(ok)) break;
    __builtin_amdgcn_s_sleep(4);
  }
  lds_wait<4>(smem, Y_CVD, 1u);
#pragma unroll
  for (int i = 0; i < 4; ++i) {
    char* yb = smem + L_YL + (lane & 15) * 4 * YROW + (lw * 16 + i * 4 + (lane >> 4)) * 2;
    *(u16*)(yb) = (u16)(yv[i][0] & 0xffffu);
    *(u16*)(yb + YROW) = (u16)(yv[i][0] >> 16);
    *(u16*)(yb + 2 * YROW) = (u16)(yv[i][2] & 0xffffu);
    *(u16*)(yb + 3 * YROW) = (u16)(yv[i][2] >> 16);
  }
}

DI void epoch_advance(const Params& P, int lane, u64 tag) {
  const __amdgpu_buffer_rsrc_t rws = mkrs(P.ws, WS_END);
  for (unsigned spins = 0; spins < SPIN_CAP; ++spins) {
    bool ok = true;
#pragma unroll
    for (int i = 0; i < 4; ++i) {
      const u32x4 g = __builtin_amdgcn_raw_buffer_load_b128(rws, WS_KVFLAG + lane * 16, i * 1024, 16);
      ok = ok && g[0] == (unsigned)tag && g[1] == (unsigned)(tag >> 32) && g[2] == (unsigned)tag && g[3] == (unsigned)(tag >> 32);
    }
    if (__all(ok)) break;
    __builtin_amdgcn_s_sleep(16);
  }
  if (lane == 0) set_tag(P.ws + WS_EPOCH, tag);
}

struct FinRegs { f32x2 x[4], xn[4]; int zo, yo; u16* dst; };
DI void final_prepare(const Params& P, int rt, int ct, int tid, u32x4 xv, unsigned sw, FinRegs& R) {
  const int s = tid >> 3, dc = tid & 7;
  float r = __builtin_amdgcn_rcpf(hi16(sw));
  const float nm = -lo16(sw);
  asm("s_nop 0" : "+v"(r));
#pragma unroll
  for (int j = 0; j < 4; ++j) {
    R.x[j] = f32x2{lo16(xv[j]), hi16(xv[j])};
    const unsigned d = pk2(add1(R.x[j][0], nm), add1(R.x[j][1], nm));
    const unsigned n = pk2(mul1(lo16(d), r), mul1(hi16(d), r));
    R.xn[j] = f32x2{lo16(n), hi16(n)};
    asm volatile("" : "+v"(R.x[j]), "+v"(R.xn[j]));
  }
  R.zo = L_Z + s * ZROW + dc * 32; R.yo = L_YL + s * YROW + dc * 16;
  R.dst = P.out + (rt * 64 + s) * D + ct * 64 + dc * 8;
  asm volatile("" : "+v"(R.zo), "+v"(R.yo), "+v"(R.dst));
}
DI void final_phase(char* smem, const FinRegs& R) {
  const char* zp = smem + R.zo;
  const f32x4 a0 = *(const f32x4*)zp, b0 = *(const f32x4*)(zp + 64 * ZROW), a1 = *(const f32x4*)(zp + 16), b1 = *(const f32x4*)(zp + 64 * ZROW + 16);
  const u32x4 yv = *(const u32x4*)(smem + R.yo);
  f32x2 z[4] = {{a0[0] + b0[0], a0[1] + b0[1]}, {a0[2] + b0[2], a0[3] + b0[3]}, {a1[0] + b1[0], a1[1] + b1[1]}, {a1[2] + b1[2], a1[3] + b1[3]}};
  f32x2 e[4];
#pragma unroll
  for (int j = 0; j < 4; ++j) e[j] = z[j] * f32x2{-1.442695041f, -1.442695041f};
#pragma unroll
  for (int j = 0; j < 4; ++j) e[j] = f32x2{__builtin_amdgcn_exp2f(e[j][0]), __builtin_amdgcn_exp2f(e[j][1])};
#pragma unroll
  for (int j = 0; j < 4; ++j) { e[j] += f32x2{1.f, 1.f}; z[j] *= R.xn[j] * f32x2{lo16(yv[j]), hi16(yv[j])}; }
#pragma unroll
  for (int j = 0; j < 4; ++j) e[j] = f32x2{__builtin_amdgcn_rcpf(e[j][0]), __builtin_amdgcn_rcpf(e[j][1])};
  u32x4 w;
#pragma unroll
  for (int j = 0; j < 4; ++j) {
    const f32x2 o = __builtin_elementwise_fma(z[j], e[j], R.x[j]);
    w[j] = pk2(o[0], o[1]);
  }
  *(__attribute__((address_space(1))) u32x4*)R.dst = w;
}

__global__ __launch_bounds__(512, 2) void fused_kernel(Params P0) {
#if __HIP_DEVICE_COMPILE__
  unsigned cjunk;
  {
    u64 code0;
    const unsigned off = ((blockIdx.x & ~7u) + (threadIdx.x >> 6)) * 256 + (threadIdx.x & 1) * 128;
    asm volatile("s_getpc_b64 %1\n.Lfk_begin:\n\tv_min_u32 %0, .Lfk_end-.Lfk_begin, %2\n\tglobal_load_dword %0, %0, %1"
                 : "=&v"(cjunk), "=&s"(code0) : "v"(off) : "memory");
    SB0();
  }
  const Params& P = P0;
  extern __shared__ __attribute__((aligned(16))) char smem[];
  const int tid = threadIdx.x, lane = tid & 63, wave = __builtin_amdgcn_readfirstlane(tid >> 6);
  const int b = blockIdx.x, xcd = b & 7, idx = b >> 3;
  const bool extra = idx >= 28;
  const int p = (xcd >> 1) * 4 + idx / 7, c = (xcd & 1) * 7 + idx % 7;
  const int rt = xcd * 4 + (idx >> 3), ct = idx & 7, d0 = 2 * b, co = ct * 128 + (rt & 1) * 32768;
  u32x4 wl[16];
  if (wave < 4) {
    if (wave == 0) *syw(smem, lane) = 0u;
    BAR();
    if (!extra) g1_compute(P, smem, p, c, wave, lane);
    else {
      {
        const unsigned t0_ = *(const unsigned*)((const char*)P.q + ((xcd >> 1) * 512 + ((idx - 28) * 4 + wave) * 32 + (lane >> 1)) * 1024 +
                                                (((xcd & 6) + (xcd & 1) * 4 + 2 + (lane & 1)) & 7) * 128);
        asm volatile("" :: "v"(t0_));
      }
      const int slow = ((384 - (int)(size_t)P.wd) >> 7) & 7, r0 = ((idx - 28) * 4 + wave) * 112;
      const char* wq = (const char*)P.wd + slow * 128;
      const char* sq = (const char*)P.ssmk + (xcd * 4 + idx - 28) * 65536 + wave * 16384 + lane * 128;
      unsigned w_[4];
      {
        const char* m_ = (const char*)P.wd + (xcd * 4 + idx - 28) * 57344 + wave * 14336 + lane * 128;
        w_[2] = *(const unsigned*)m_; w_[3] = *(const unsigned*)(m_ + (lane < 48 ? 8192 : 0));
        asm volatile("" :: "v"(w_[2]), "v"(w_[3]));
      }
      w_[0] = *(const unsigned*)(wq + (r0 + lane) * 1024);
      w_[1] = *(const unsigned*)(wq + (r0 + 64 + (lane < 48 ? lane : 0)) * 1024);
      asm volatile("" :: "v"(w_[0]), "v"(w_[1]));
      lds_wait<2, 4>(smem, Y_EPI, 1u);
      w_[2] = *(const unsigned*)sq; w_[3] = *(const unsigned*)(sq + 8192);
      asm volatile("" :: "v"(w_[2]), "v"(w_[3]));
    }
    lds_set(smem, Y_DONE1 + wave, 10u, lane);
  } else if (wave < 6) {
    __builtin_amdgcn_s_setprio(3);
    if (!extra) g1_dma(P, smem, p, c, (xcd & 6) + (xcd & 1) * 4, wave - 4, lane, co, wl);
    else {
      const __amdgpu_buffer_rsrc_t rwd = mkrs(P.wd, D * F * 2);
      const int vwd = wd_voff(lane);
      {
        const int i_ = ((idx - 28) * 2 + wave - 4) * 32 + (lane & 31);
        const char* a_ = i_ < 16 ? (const char*)P.q + i_ * 131072 : i_ < 32 ? (const char*)P.k + (i_ - 16) * 131072 : i_ < 48 ? (const char*)P.v + (i_ - 32) * 131072
                       : i_ < 64 ? (const char*)P.ssmk + (i_ - 48) * 131072 : i_ < 80 ? (const char*)P.out + (i_ - 64) * 131072 : i_ < 94 ? (const char*)P.wg + (i_ - 80) * 131072
                       : i_ < 108 ? (const char*)P.wu + (i_ - 94) * 131072 : i_ < 122 ? (const char*)P.wd + (i_ - 108) * 131072 : i_ < 218 ? P.ws + (i_ - 122) * 163840 : P.ws;
        const u64 t_ = __hip_atomic_load((const gu64*)a_, RLX_AGENT);
        asm volatile("" :: "v"(t_));
      }
      RAWBAR();
      lds_wait<2, 4>(smem, Y_EPI, 1u);
#pragma unroll
      for (int j = 0; j < 7; ++j) wd_stage(rwd, smem, (j + 6) & 7, wave - 4, vwd, co);
      WAITV(32);
      wd_stage(rwd, smem, 5, wave - 4, vwd, co);
      WAITV(0); lds_set(smem, Y_LAND1 + wave - 4, 17u, lane);
      late_load(P, co, wave - 4, lane, wl);
      LATE_WAIT(wl);
    }
  } else {
    __builtin_amdgcn_s_setprio(3);
    u64 tg = read_epoch(P);
    const int sw = wave - 6;
    KvRegs R;
    u32x4 xr[4];
    kv_load(P, b, sw, lane, R);
#pragma unroll
    for (int i = 0; i < 4; ++i) xr[i] = *(const u32x4*)(P.q + (b * 8 + sw * 4 + i) * D + lane * 8);
    SB0();
    RAWBAR();
    PIN_TAG(tg);
    if (sw == 0 && lane == 0) *(u64*)(smem + L_TAG) = tg;
    row_stats(P, b, sw, lane, tg, xr);
    kv_half(smem, b, sw, lane, R, 0);
    kv_half(smem, b, sw, lane, R, 1);
    kv_store(P, smem, b, sw, lane);
    if (!extra) {
      EpiRegs E;
      cs_publish(P, smem, p, c, sw, lane, tg);
      kv_publish(P, b, sw, lane, tg);
      lds_wait<2, 4>(smem, Y_LAND1, 12u);
      epi_load(P, p, c, sw, lane, E);
      epi_tables(P, smem, p, c, sw, lane, tg, E);
    } else {
      kv_publish(P, b, sw, lane, tg);
      lds_set(smem, Y_EPI + sw, 1u, lane);
    }
    late_load(P, co, wave - 4, lane, wl);
    side_stage(P, smem, d0, sw, lane, extra);
    LATE_WAIT(wl);
  }
  XRegs X;
  FinRegs FR;
  if (wave < 4) {
    if (extra) lds_wait<1>(smem, Y_KV, 1u);
    const u64 tag = lds_tag(smem);
    unsigned path = 2u;
    if (!extra) {
      char* myflag = P.ws + WS_HFLAG + ((p * 2 + (wave & 1)) * 64 + 32 + c * 2 + (wave >> 1)) * HFS;
      if (wave == 0) {
        u32x2 f;
        asm volatile("s_nop 4\n\tglobal_load_dwordx2 %0, %1, %2 sc1\n\ts_waitcnt vmcnt(1)"
                     : "=&v"(f) : "v"(WS_HFLAG + (rt * 64 + (lane < 28 ? lane : 0)) * HFS), "s"(P.ws) : "memory");
        if (lane == 0) set_tag(myflag, tag);
        asm volatile("s_waitcnt vmcnt(1)" : "+v"(f) :: "memory");
        path = __all(f[0] == (unsigned)tag && f[1] == (unsigned)(tag >> 32)) ? 1u : 2u;
        lds_set(smem, Y_PATH, path, lane);
      } else {
        WAITV(0);
        if (lane == 0) set_tag(myflag, tag);
        lds_wait<1, 0>(smem, Y_PATH, 1u);
        path = lds_look<1>(smem, Y_PATH);
      }
    }
    if (path != 1u) {
      if (wave == 0) {
        poll_tags<4>(P.ws + WS_HFLAG + rt * 64 * HFS, 28, HFS / 8, tag, lane);
        lds_set(smem, Y_HSEEN, 1u, lane);
      } else lds_wait<1>(smem, Y_HSEEN, 1u);
    }
    __builtin_amdgcn_s_setprio(2);
    g2_compute(P, smem, rt, ct, wave, lane, tid, tag, X);
    final_prepare(P, rt, ct, tid, X.x, x_settle(P, rt, tid, X, tag), FR);
  } else {
    x_load(P, rt, ct, tid, X);
    conv_phase(P, smem, d0, wave - 4, lane);
    lds_set(smem, Y_CVD + wave - 4, 1u, lane);
    const u64 tag = lds_tag(smem);
    late_store(P, smem, co, wave - 4, lane, wl);
    final_prepare(P, rt, ct, tid, X.x, x_settle(P, rt, tid, X, tag), FR);
    if (b == 224 && wave == 4) epoch_advance(P, lane, tag);
    y_tile(P, smem, rt, ct, wave - 4, lane, tag);
  }
  BAR();
  final_phase(smem, FR);
  asm volatile(".Lfk_end:" :: "v"(cjunk));
#endif
}
}

extern "C" void kernel_launch(void* const* d_in, const int* in_sizes, int n_in, void* d_out, int out_size, void* d_ws,
                              size_t ws_size, hipStream_t stream) {
  (void)in_sizes; (void)n_in; (void)out_size; (void)ws_size;
  Params P;
  P.q = (const u16*)d_in[0]; P.k = (const u16*)d_in[1]; P.v = (const u16*)d_in[2]; P.sk = (const u16*)d_in[3];
  P.ssmk = (const u16*)d_in[4]; P.ssmd = (const u16*)d_in[5];
  P.wg = (const u16*)d_in[7]; P.wu = (const u16*)d_in[8]; P.wd = (const u16*)d_in[9];
  P.out = (u16*)d_out; P.ws = (char*)d_ws;
  (void)hipFuncSetAttribute((const void*)fused_kernel, hipFuncAttributeMaxDynamicSharedMemorySize, LDS_BYTES);
  fused_kernel<<<256, 512, LDS_BYTES, stream>>>(P);
}
```
